```python
import torch

def _softmax(x, axis=-1):
    return torch.softmax(x, dim=axis)
def _gelu(x):
    return torch.nn.functional.gelu(x, approximate="tanh")
def _silu(x):
    return torch.nn.functional.silu(x)
def _sigmoid(x):
    return torch.sigmoid(x)
def _layernorm(x, axis=-1, eps=1e-5):
    m = x.mean(dim=axis, keepdim=True)
    v = x.var(dim=axis, keepdim=True, unbiased=False)
    return (x - m) / torch.sqrt(v + eps)
def _scatter_add(tgt, idx, src, axis=0):
    return tgt.clone().index_add_(axis, idx, src)
def _segment_sum(src, seg, n):
    out = torch.zeros((n,) + src.shape[1:], dtype=src.dtype)
    return out.index_add_(0, seg, src)
def _pad(x, pw):
    flat = tuple(v for pair in reversed(pw) for v in pair)
    return torch.nn.functional.pad(x, flat)

kernel_name = 'recurrent_scan__sliding_block__glu_gated_geglu__prompt'

def setup_inputs():

    _w = (lambda a: (lambda t: t.to(torch.bfloat16) if t.is_floating_point() else t)(torch.as_tensor(a)))
    def _A_setup():
        B, S, D = 1, 2048, 512
        return {
            "x": _w((lambda *s: torch.randn(*s, dtype=torch.bfloat16) * 0.05)(B, S, D)),
            "wqkv": _w((lambda *s: torch.randn(*s, dtype=torch.bfloat16) * 0.2)(D, 3 * D)),
            "wo": _w((lambda *s: torch.randn(*s, dtype=torch.bfloat16) * 0.2)(D, D)),
        }
    _A_in = _A_setup()

    def _B_setup():
        B, S, Dm, Df = 1, 2048, 512, 1792
        return {
            "x": _w((lambda *s: torch.randn(*s, dtype=torch.bfloat16) * 0.05)(B, S, Dm)),
            "w_gate": _w((lambda *s: torch.randn(*s, dtype=torch.bfloat16) * 0.1)(Dm, Df) * (Dm ** -0.5)),
            "w_up": _w((lambda *s: torch.randn(*s, dtype=torch.bfloat16) * 0.1)(Dm, Df) * (Dm ** -0.5)),
            "w_down": _w((lambda *s: torch.randn(*s, dtype=torch.bfloat16) * 0.1)(Df, Dm) * (Df ** -0.5)),
        }
    _B_in = _B_setup()

    return {
        "A_x": _A_in["x"],
        "A_wqkv": _A_in["wqkv"],
        "A_wo": _A_in["wo"],
        "B_x": _B_in["x"],
        "B_w_gate": _B_in["w_gate"],
        "B_w_up": _B_in["w_up"],
        "B_w_down": _B_in["w_down"],
    }

def reference(A_x, A_wqkv, A_wo, B_x, B_w_gate, B_w_up, B_w_down):
    def _A_ref(x, wqkv, wo):
        B, S, D = x.shape
        H = 32; Dh = D // H; W = 256
        qkv = (lambda x, *d: x.permute(*d))((x @ wqkv).reshape(B, S, 3, H, Dh), 2, 0, 3, 1, 4)
        q, k, v = qkv[0], qkv[1], qkv[2]
        scores = torch.einsum("bhqd,bhkd->bhqk", q, k) * (Dh ** -0.5)
        idx = torch.arange(S)
        mask = (idx[:, None] - idx[None, :] >= 0) & (idx[:, None] - idx[None, :] < W)
        scores = torch.where(mask[None, None], scores, -1e9)
        p = _softmax(scores, axis=-1)
        o = torch.einsum("bhqk,bhkd->bhqd", p, v)
        return (lambda x, *d: x.permute(*d))(o, 0, 2, 1, 3).reshape(B, S, D) @ wo

    def _B_ref(x, w_gate, w_up, w_down):
        g = torch.einsum("bsd,df->bsf", x, w_gate)
        u = torch.einsum("bsd,df->bsf", x, w_up)
        h = _gelu(g) * u
        return torch.einsum("bsf,fd->bsd", h, w_down)

    x = A_x
    xn = _layernorm(x, axis=-1)
    s = _A_ref(xn, A_wqkv, A_wo)
    g = _silu(_B_ref(xn, B_w_gate, B_w_up, B_w_down))
    return x + s * g
```

```cpp
#include <hip/hip_runtime.h>
#include <stdint.h>

typedef unsigned short u16;
typedef __bf16 bf16x8 __attribute__((ext_vector_type(8)));
typedef __bf16 bf16x2 __attribute__((ext_vector_type(2)));
typedef short s16x4 __attribute__((ext_vector_type(4)));
typedef short s16x8 __attribute__((ext_vector_type(8)));
typedef float f32x2 __attribute__((ext_vector_type(2)));
typedef float f32x4 __attribute__((ext_vector_type(4)));
typedef float f32x16 __attribute__((ext_vector_type(16)));
typedef unsigned u32x2 __attribute__((ext_vector_type(2)));
typedef unsigned u32x4 __attribute__((ext_vector_type(4)));
typedef __attribute__((address_space(3))) s16x4* lds_s16x4_ptr;

#define DI __device__ __forceinline__

constexpr int S_ = 2048, D_ = 512, DFF = 1792, NQKV = 1536;

DI float bf2f(u16 v) { return __uint_as_float(((unsigned)v) << 16); }
DI float bflo(unsigned p) { return __uint_as_float(p << 16); }
DI float bfhi(unsigned p) { return __uint_as_float(p & 0xffff0000u); }
DI unsigned pack2(float a, float b) {
  f32x2 f = {a, b};
  return __builtin_bit_cast(unsigned, __builtin_convertvector(f, bf16x2));
}
DI float bfr(float a) { return __uint_as_float(((unsigned)__builtin_bit_cast(u16, (__bf16)a)) << 16); }
DI bf16x8 tr_frag(const char* p) {
  s16x4 lo = __builtin_amdgcn_ds_read_tr16_b64_v4i16((lds_s16x4_ptr)(p));
  s16x4 hi = __builtin_amdgcn_ds_read_tr16_b64_v4i16((lds_s16x4_ptr)(p + 512));
  s16x8 r = __builtin_shufflevector(lo, hi, 0, 1, 2, 3, 4, 5, 6, 7);
  return __builtin_bit_cast(bf16x8, r);
}
DI void glds16(const void* g, void* l) {
  __builtin_amdgcn_global_load_lds((const __attribute__((address_space(1))) void*)g,
                                   (__attribute__((address_space(3))) void*)l, 16, 0, 0);
}
DI float dpp_add(float v, const int ctrl_sel) {
  int t;
  if (ctrl_sel == 0) t = __builtin_amdgcn_mov_dpp(__float_as_int(v), 0xB1, 0xf, 0xf, true);
  else if (ctrl_sel == 1) t = __builtin_amdgcn_mov_dpp(__float_as_int(v), 0x4E, 0xf, 0xf, true);
  else t = __builtin_amdgcn_mov_dpp(__float_as_int(v), 0x141, 0xf, 0xf, true);
  return v + __int_as_float(t);
}
#define WAIT_VM(n) asm volatile("s_waitcnt vmcnt(%0)" ::"i"(n) : "memory")
#define BARRIER() do { asm volatile("s_waitcnt lgkmcnt(0)" ::: "memory"); __builtin_amdgcn_s_barrier(); asm volatile("" ::: "memory"); } while (0)

DI int wsw(int r) { return ((r >> 1) & 1) | (((r >> 3) & 1) << 1); }

__global__ __launch_bounds__(512, 2) void k1_ln_gemm(const u16* __restrict__ x, const u16* __restrict__ wqkv,
                                                     const u16* __restrict__ wg, const u16* __restrict__ wu,
                                                     u16* __restrict__ qkv, u16* __restrict__ hout) {
  __shared__ __attribute__((aligned(16))) char smem[2 * 16384 + 2 * 40960];
  char* const Abuf = smem;
  char* const Bbuf = smem + 32768;
  const int tid = threadIdx.x, w = tid >> 6, l = tid & 63;
  const int bid = blockIdx.x, xcd = bid & 7, ii = bid >> 3;
  const int mt = (xcd & 1) * 8 + (ii & 7), nt = (xcd >> 1) * 4 + (ii >> 3);
  const int m0 = mt * 128;

  const u16* wsrc[5];
  int wld[5];
  {
    const int r = 8 * w + (l >> 3), p = l & 7;
    const int chunk = ((((p >> 1) ^ wsw(r)) << 1) | (p & 1));
#pragma unroll
    for (int pl = 0; pl < 5; ++pl) {
      const int P = nt * 5 + pl;
      if (P < 24) {
        wld[pl] = NQKV;
        wsrc[pl] = wqkv + (size_t)r * NQKV + 64 * P + 8 * chunk;
      } else {
        wld[pl] = DFF;
        const u16* b = chunk < 4 ? wg : wu;
        wsrc[pl] = b + (size_t)r * DFF + 32 * (P - 24) + 8 * (chunk & 3);
      }
    }
  }
  auto stageW = [&](int t) {
    char* dst = Bbuf + (t & 1) * 40960 + w * 1024;
#pragma unroll
    for (int pl = 0; pl < 5; ++pl) {
      glds16(wsrc[pl] + (size_t)t * 64 * wld[pl], dst + pl * 8192);
    }
  };
  stageW(0);

  uint4 xr[2][8];
#pragma unroll
  for (int g = 0; g < 2; ++g) {
    const u16* xp = x + (size_t)(m0 + 16 * w + 8 * g + (l >> 3)) * D_ + 8 * (l & 7);
#pragma unroll
    for (int i = 0; i < 8; ++i) xr[g][i] = *(const uint4*)(xp + 64 * i);
  }
  float lsc[2], lsh[2];
#pragma unroll
  for (int g = 0; g < 2; ++g) {
    float s1 = 0.f, s2 = 0.f;
    const bf16x2 ones = {(__bf16)1.0f, (__bf16)1.0f};
#pragma unroll
    for (int i = 0; i < 8; ++i) {
      const unsigned d[4] = {xr[g][i].x, xr[g][i].y, xr[g][i].z, xr[g][i].w};
#pragma unroll
      for (int e = 0; e < 4; ++e) {
        bf16x2 v = __builtin_bit_cast(bf16x2, d[e]);
        s1 = __builtin_amdgcn_fdot2_f32_bf16(v, ones, s1, false);
        s2 = __builtin_amdgcn_fdot2_f32_bf16(v, v, s2, false);
      }
    }
    s1 = dpp_add(s1, 0); s1 = dpp_add(s1, 1); s1 = dpp_add(s1, 2);
    s2 = dpp_add(s2, 0); s2 = dpp_add(s2, 1); s2 = dpp_add(s2, 2);
    const float mean = s1 * (1.0f / D_);
    float var = s2 * (1.0f / D_) - mean * mean;
    var = fmaxf(var, 0.f);
    const float mb = bfr(mean), vb = bfr(var);
    const float sd = bfr(sqrtf(bfr(vb + 1e-5f)));
    const float r = 1.0f / sd;
    lsc[g] = r;
    lsh[g] = -mb * r;
  }
  auto stageA = [&](int t, const uint4 v0, const uint4 v1) {
    char* dst = Abuf + (t & 1) * 16384;
#pragma unroll
    for (int g = 0; g < 2; ++g) {
      const uint4 v = g ? v1 : v0;
      const float sc = lsc[g], sh = lsh[g];
      uint4 o;
      o.x = pack2(bflo(v.x) * sc + sh, bfhi(v.x) * sc + sh);
      o.y = pack2(bflo(v.y) * sc + sh, bfhi(v.y) * sc + sh);
      o.z = pack2(bflo(v.z) * sc + sh, bfhi(v.z) * sc + sh);
      o.w = pack2(bflo(v.w) * sc + sh, bfhi(v.w) * sc + sh);
      const int row = 16 * w + 8 * g + (l >> 3);
      *(uint4*)(dst + row * 128 + (((l & 7) ^ (l >> 3)) << 4)) = o;
    }
  };
  stageA(0, xr[0][0], xr[1][0]);

  const int wm = w & 3, wn = w >> 2;
  int xoff[2];
#pragma unroll
  for (int kk = 0; kk < 2; ++kk) xoff[kk] = (32 * wm + (l & 15)) * 128 + (((4 * kk + (l >> 4)) ^ (l & 7)) << 4);
  int woff[10];
  {
    const int g = l >> 4, q = (l & 15) >> 2, p = l & 3;
    const int sl = ((q >> 1) & 1) | ((g & 1) << 1);
    const int base = (8 * g + q) * 128 + 8 * p;
#pragma unroll
    for (int j = 0; j < 10; ++j) {
      const int u = 5 * wn + (j >> 1), pl = u >> 1, half = u & 1, e = j & 1;
      const bool isq = (nt * 5 + pl) < 24;
      const int c = isq ? (2 * half + e) : (half + 2 * e);
      woff[j] = pl * 8192 + base + ((c ^ sl) << 5);
    }
  }
  f32x4 acc[2][10];
#pragma unroll
  for (int mi = 0; mi < 2; ++mi)
#pragma unroll
    for (int j = 0; j < 10; ++j) acc[mi][j] = f32x4{0.f, 0.f, 0.f, 0.f};

  WAIT_VM(0);
  BARRIER();

#pragma unroll
  for (int t = 0; t < 8; ++t) {
    if (t < 7) {
      stageW(t + 1);
      stageA(t + 1, xr[0][(t + 1) & 7], xr[1][(t + 1) & 7]);
    }
    const char* a = Abuf + (t & 1) * 16384;
    const char* b = Bbuf + (t & 1) * 40960;
#pragma unroll
    for (int kk = 0; kk < 2; ++kk) {
      bf16x8 xf[2];
#pragma unroll
      for (int mi = 0; mi < 2; ++mi) xf[mi] = *(const bf16x8*)(a + xoff[kk] + mi * 2048);
#pragma unroll
      for (int j = 0; j < 10; ++j) {
        const bf16x8 wf = tr_frag(b + woff[j] + kk * 4096);
#pragma unroll
        for (int mi = 0; mi < 2; ++mi) acc[mi][j] = __builtin_amdgcn_mfma_f32_16x16x32_bf16(wf, xf[mi], acc[mi][j], 0, 0, 0);
      }
    }
    WAIT_VM(0);
    BARRIER();
  }

#pragma unroll
  for (int uu = 0; uu < 5; ++uu) {
    const int U = nt * 10 + 5 * wn + uu, P = U >> 1, half = U & 1;
    if (P < 24) {
#pragma unroll
      for (int e = 0; e < 2; ++e) {
        const int col = 64 * P + 32 * half + 16 * e;
        u16* dst = qkv + (size_t)(col >> 4) * (S_ * 16) + 4 * (l >> 4);
#pragma unroll
        for (int mi = 0; mi < 2; ++mi) {
          const f32x4 v = acc[mi][2 * uu + e];
          const int tok = m0 + 32 * wm + 16 * mi + (l & 15);
          u32x2 o = {pack2(v[0], v[1]), pack2(v[2], v[3])};
          *(u32x2*)(dst + tok * 16) = o;
        }
      }
    } else {
      const int hcol = 32 * (P - 24) + 16 * half + 4 * (l >> 4);
#pragma unroll
      for (int mi = 0; mi < 2; ++mi) {
        const f32x4 gv = acc[mi][2 * uu], uv = acc[mi][2 * uu + 1];
        float hv[4];
#pragma unroll
        for (int r = 0; r < 4; ++r) {
          const float gg = bfr(gv[r]), up = bfr(uv[r]);
          const float z = 0.7978845608028654f * (gg + 0.044715f * gg * gg * gg);
          const float e2 = __builtin_amdgcn_exp2f(-2.0f * 1.4426950408889634f * z);
          const float ge = bfr(gg * __builtin_amdgcn_rcpf(1.0f + e2));
          hv[r] = ge * up;
        }
        const int tok = m0 + 32 * wm + 16 * mi + (l & 15);
        u32x2 o = {pack2(hv[0], hv[1]), pack2(hv[2], hv[3])};
        *(u32x2*)(hout + (size_t)tok * DFF + hcol) = o;
      }
    }
  }
}

template <int K, int NS>
DI void gemm64(const u16* __restrict__ A, const u16* __restrict__ B, int m0, int n0, char* smem, f32x4 (&acc)[2][2]) {
  const int tid = threadIdx.x, w = tid >> 6, l = tid & 63;
  constexpr int NSTEP = K / 64;
  const u16* asrc = A + (size_t)(m0 + 8 * w + (l >> 3)) * K + 8 * ((l & 7) ^ (l >> 3));
  const u16* bsrc;
  {
    const int r = 8 * w + (l >> 3), p = l & 7;
    const int chunk = ((((p >> 1) ^ wsw(r)) << 1) | (p & 1));
    bsrc = B + (size_t)r * D_ + n0 + 8 * chunk;
  }
  auto stage = [&](int s) {
    char* dst = smem + (s % NS) * 16384 + w * 1024;
    glds16(asrc + s * 64, dst);
    glds16(bsrc + (size_t)s * 64 * D_, dst + 8192);
  };
#pragma unroll
  for (int s = 0; s < NS - 1; ++s) stage(s);

  const int kh = w >> 2, wq = w & 3, wm = wq & 1, wn = wq >> 1;
  const int xoff = (32 * wm + (l & 15)) * 128 + (((4 * kh + (l >> 4)) ^ (l & 7)) << 4);
  int woff[2];
  {
    const int g = l >> 4, q = (l & 15) >> 2, p = l & 3;
    const int sl = ((q >> 1) & 1) | ((g & 1) << 1);
#pragma unroll
    for (int nj = 0; nj < 2; ++nj) woff[nj] = 8192 + (32 * kh + 8 * g + q) * 128 + 8 * p + (((2 * wn + nj) ^ sl) << 5);
  }
#pragma unroll
  for (int mi = 0; mi < 2; ++mi)
#pragma unroll
    for (int nj = 0; nj < 2; ++nj) acc[mi][nj] = f32x4{0.f, 0.f, 0.f, 0.f};

  for (int s = 0; s < NSTEP; ++s) {
    if (s + NS - 2 < NSTEP) { WAIT_VM(2 * (NS - 2)); } else { WAIT_VM(0); }
    BARRIER();
    if (s + NS - 1 < NSTEP) stage(s + NS - 1);
    const char* buf = smem + (s % NS) * 16384;
    bf16x8 xf[2], wf[2];
#pragma unroll
    for (int mi = 0; mi < 2; ++mi) xf[mi] = *(const bf16x8*)(buf + xoff + mi * 2048);
#pragma unroll
    for (int nj = 0; nj < 2; ++nj) wf[nj] = tr_frag(buf + woff[nj]);
#pragma unroll
    for (int mi = 0; mi < 2; ++mi)
#pragma unroll
      for (int nj = 0; nj < 2; ++nj) acc[mi][nj] = __builtin_amdgcn_mfma_f32_16x16x32_bf16(wf[nj], xf[mi], acc[mi][nj], 0, 0, 0);
  }
  BARRIER();
  if (kh == 1) {
#pragma unroll
    for (int mi = 0; mi < 2; ++mi)
#pragma unroll
      for (int nj = 0; nj < 2; ++nj) *(f32x4*)(smem + ((wq * 4 + mi * 2 + nj) * 64 + l) * 16) = acc[mi][nj];
  }
  BARRIER();
  if (kh == 0) {
#pragma unroll
    for (int mi = 0; mi < 2; ++mi)
#pragma unroll
      for (int nj = 0; nj < 2; ++nj) acc[mi][nj] += *(const f32x4*)(smem + ((wq * 4 + mi * 2 + nj) * 64 + l) * 16);
  }
}

__global__ __launch_bounds__(512, 4) void k2_attn_down(const u16* __restrict__ qkv, const u16* __restrict__ h,
                                                       const u16* __restrict__ wdown, u16* __restrict__ o,
                                                       u16* __restrict__ gout) {
  __shared__ __attribute__((aligned(16))) char smem[65536];
  const int tid = threadIdx.x, w = tid >> 6, l = tid & 63;
  if (blockIdx.x < 256) {
    const int head = blockIdx.x >> 3, qc = blockIdx.x & 7;
    const u16* Q = qkv + (size_t)head * (S_ * 16);
    const u16* Kp = qkv + (size_t)(32 + head) * (S_ * 16);
    const u16* Vp = qkv + (size_t)(64 + head) * (S_ * 16);
#pragma unroll
    for (int i = 0; i < 4; ++i) {
      const int n = w * 4 + i, rb = n & 15;
      int key = 256 * qc - 256 + 32 * rb + (l >> 1);
      key = key < 0 ? 0 : key;
      const u16* src = (n < 16 ? Kp : Vp) + key * 16 + 8 * (l & 1);
      glds16(src, smem + n * 1024);
    }
    const int r = l & 31, hh = l >> 5;
    const int q0 = 256 * qc + 32 * w;
    const bf16x8 qf = *(const bf16x8*)(Q + (q0 + r) * 16 + 8 * hh);
    WAIT_VM(0);
    BARRIER();

    const float c = 0.25f * 1.4426950408889634f;
    float m = -1e30f;
    f32x16 oacc;
#pragma unroll
    for (int i = 0; i < 16; ++i) oacc[i] = 0.f;
    const int t0 = (qc == 0) ? (8 - w) : 0;
    const char* kbase = smem + (32 * w + r) * 32 + 16 * hh;
    const char* vbase = smem + 16384 + (32 * w + 4 * hh + ((l & 15) >> 2)) * 32 + 8 * (l & 3);
    const bool isd = (l & 16) == 0;
    for (int t = t0; t <= 8; ++t) {
      const bf16x8 kf = *(const bf16x8*)(kbase + t * 1024);
      f32x16 s;
#pragma unroll
      for (int i = 0; i < 16; ++i) s[i] = 0.f;
      s = __builtin_amdgcn_mfma_f32_32x32x16_bf16(kf, qf, s, 0, 0, 0);
      if (t == 0) {
#pragma unroll
        for (int i = 0; i < 16; ++i) { const int kk = (i & 3) + 8 * (i >> 2) + 4 * hh; s[i] = (kk > r) ? s[i] : -INFINITY; }
      } else if (t == 8) {
#pragma unroll
        for (int i = 0; i < 16; ++i) { const int kk = (i & 3) + 8 * (i >> 2) + 4 * hh; s[i] = (kk <= r) ? s[i] : -INFINITY; }
      }
      float tm = s[0];
#pragma unroll
      for (int i = 1; i < 16; ++i) tm = fmaxf(tm, s[i]);
      tm = fmaxf(tm, __shfl_xor(tm, 32));
      const float mn = fmaxf(m, tm);
      const float alpha = __builtin_amdgcn_exp2f((m - mn) * c);
      m = mn;
      const float mc = mn * c;
      unsigned pp[8];
#pragma unroll
      for (int i = 0; i < 8; ++i)
        pp[i] = pack2(__builtin_amdgcn_exp2f(s[2 * i] * c - mc), __builtin_amdgcn_exp2f(s[2 * i + 1] * c - mc));
#pragma unroll
      for (int i = 0; i < 16; ++i) oacc[i] *= alpha;
#pragma unroll
      for (int st = 0; st < 2; ++st) {
        const char* vp = vbase + t * 1024 + st * 512;
        s16x4 lo = __builtin_amdgcn_ds_read_tr16_b64_v4i16((lds_s16x4_ptr)(vp));
        s16x4 hi = __builtin_amdgcn_ds_read_tr16_b64_v4i16((lds_s16x4_ptr)(vp + 256));
        s16x8 vv = __builtin_shufflevector(lo, hi, 0, 1, 2, 3, 4, 5, 6, 7);
        s16x8 one8 = {0x3F80, 0x3F80, 0x3F80, 0x3F80, 0x3F80, 0x3F80, 0x3F80, 0x3F80};
        vv = isd ? vv : one8;
        u32x4 pw = {pp[4 * st], pp[4 * st + 1], pp[4 * st + 2], pp[4 * st + 3]};
        oacc = __builtin_amdgcn_mfma_f32_32x32x16_bf16(__builtin_bit_cast(bf16x8, vv), __builtin_bit_cast(bf16x8, pw), oacc, 0, 0, 0);
      }
    }
    const float inv = 1.0f / oacc[8];
    u16* dst = o + (size_t)(q0 + r) * D_ + head * 16 + 4 * hh;
    u32x2 o0 = {pack2(oacc[0] * inv, oacc[1] * inv), pack2(oacc[2] * inv, oacc[3] * inv)};
    u32x2 o1 = {pack2(oacc[4] * inv, oacc[5] * inv), pack2(oacc[6] * inv, oacc[7] * inv)};
    *(u32x2*)(dst) = o0;
    *(u32x2*)(dst + 8) = o1;
  } else {
    const int b = blockIdx.x - 256, xcd = b & 7, ii = b >> 3;
    const int mt = xcd * 4 + (ii & 3), nt = ii >> 2;
    f32x4 acc[2][2];
    gemm64<DFF, 4>(h, wdown, mt * 64, nt * 64, smem, acc);
    if (w < 4) {
      const int wm = w & 1, wn = w >> 1;
#pragma unroll
      for (int mi = 0; mi < 2; ++mi)
#pragma unroll
        for (int nj = 0; nj < 2; ++nj) {
          float gv[4];
#pragma unroll
          for (int r = 0; r < 4; ++r) {
            const float d = bfr(acc[mi][nj][r]);
            gv[r] = d * __builtin_amdgcn_rcpf(1.0f + __builtin_amdgcn_exp2f(-1.4426950408889634f * d));
          }
          const int tok = mt * 64 + 32 * wm + 16 * mi + (l & 15), col = nt * 64 + 32 * wn + 16 * nj + 4 * (l >> 4);
          u32x2 ov = {pack2(gv[0], gv[1]), pack2(gv[2], gv[3])};
          *(u32x2*)(gout + (size_t)tok * D_ + col) = ov;
        }
    }
  }
}

__global__ __launch_bounds__(512, 2) void k3_wo_final(const u16* __restrict__ o, const u16* __restrict__ wo,
                                                      const u16* __restrict__ x, const u16* __restrict__ g,
                                                      u16* __restrict__ out) {
  __shared__ __attribute__((aligned(16))) char smem[65536];
  const int tid = threadIdx.x, w = tid >> 6, l = tid & 63;
  const int b = blockIdx.x, xcd = b & 7, ii = b >> 3;
  const int mt = xcd * 4 + (ii & 3), nt = ii >> 2;
  f32x4 acc[2][2];
  gemm64<D_, 4>(o, wo, mt * 64, nt * 64, smem, acc);
  if (w < 4) {
    const int wm = w & 1, wn = w >> 1;
#pragma unroll
    for (int mi = 0; mi < 2; ++mi)
#pragma unroll
      for (int nj = 0; nj < 2; ++nj) {
        const int tok = mt * 64 + 32 * wm + 16 * mi + (l & 15), col = nt * 64 + 32 * wn + 16 * nj + 4 * (l >> 4);
        const size_t idx = (size_t)tok * D_ + col;
        const u32x2 xv = *(const u32x2*)(x + idx), gv = *(const u32x2*)(g + idx);
        const float xs[4] = {bflo(xv[0]), bfhi(xv[0]), bflo(xv[1]), bfhi(xv[1])};
        const float gs[4] = {bflo(gv[0]), bfhi(gv[0]), bflo(gv[1]), bfhi(gv[1])};
        float ov[4];
#pragma unroll
        for (int r = 0; r < 4; ++r) ov[r] = xs[r] + bfr(bfr(acc[mi][nj][r]) * gs[r]);
        u32x2 o2 = {pack2(ov[0], ov[1]), pack2(ov[2], ov[3])};
        *(u32x2*)(out + idx) = o2;
      }
  }
}

extern "C" void kernel_launch(void* const* d_in, const int* in_sizes, int n_in, void* d_out, int out_size, void* d_ws,
                              size_t ws_size, hipStream_t stream) {
  const u16* x = (const u16*)d_in[0];
  const u16* wqkv = (const u16*)d_in[1];
  const u16* wo = (const u16*)d_in[2];
  const u16* wg = (const u16*)d_in[4];
  const u16* wu = (const u16*)d_in[5];
  const u16* wd = (const u16*)d_in[6];
  char* ws = (char*)d_ws;
  u16* qkv = (u16*)(ws);
  u16* h = (u16*)(ws + (6u << 20));
  u16* o = (u16*)(ws + (14u << 20));
  u16* g = (u16*)(ws + (16u << 20));
  k1_ln_gemm<<<256, 512, 0, stream>>>(x, wqkv, wg, wu, qkv, h);
  k2_attn_down<<<512, 512, 0, stream>>>(qkv, h, wd, o, g);
  k3_wo_final<<<256, 512, 0, stream>>>(o, wo, x, g, (u16*)d_out);
}
```

```cpp
#include <hip/hip_runtime.h>
#include <stdint.h>

typedef unsigned short u16;
typedef __bf16 bf16x8 __attribute__((ext_vector_type(8)));
typedef __bf16 bf16x2 __attribute__((ext_vector_type(2)));
typedef short s16x4 __attribute__((ext_vector_type(4)));
typedef short s16x8 __attribute__((ext_vector_type(8)));
typedef float f32x2 __attribute__((ext_vector_type(2)));
typedef float f32x4 __attribute__((ext_vector_type(4)));
typedef float f32x16 __attribute__((ext_vector_type(16)));
typedef unsigned u32x2 __attribute__((ext_vector_type(2)));
typedef unsigned u32x4 __attribute__((ext_vector_type(4)));
typedef __attribute__((address_space(3))) s16x4* lds_s16x4_ptr;

#define DI __device__ __forceinline__

#define K3_NS 4
constexpr int S_ = 2048, D_ = 512, DFF = 1792, NQKV = 1536;

DI float bf2f(u16 v) { return __uint_as_float(((unsigned)v) << 16); }
DI float bflo(unsigned p) { return __uint_as_float(p << 16); }
DI float bfhi(unsigned p) { return __uint_as_float(p & 0xffff0000u); }
DI unsigned pack2(float a, float b) {
  f32x2 f = {a, b};
  return __builtin_bit_cast(unsigned, __builtin_convertvector(f, bf16x2));
}
DI float bfr(float a) { return __uint_as_float(((unsigned)__builtin_bit_cast(u16, (__bf16)a)) << 16); }
DI bf16x8 tr_frag(const char* p) {
  s16x4 lo = __builtin_amdgcn_ds_read_tr16_b64_v4i16((lds_s16x4_ptr)(p));
  s16x4 hi = __builtin_amdgcn_ds_read_tr16_b64_v4i16((lds_s16x4_ptr)(p + 512));
  s16x8 r = __builtin_shufflevector(lo, hi, 0, 1, 2, 3, 4, 5, 6, 7);
  return __builtin_bit_cast(bf16x8, r);
}
DI void glds16(const void* g, const void* l) {
  unsigned keep;
  const unsigned lo = __builtin_amdgcn_readfirstlane((unsigned)(size_t)l);
  asm volatile("s_mov_b32 %0, m0\n\ts_mov_b32 m0, %2\n\ts_nop 0\n\tglobal_load_lds_dwordx4 %1, off\n\ts_mov_b32 m0, %0"
               : "=&s"(keep) : "v"(g), "s"(lo) : "memory");
}
DI float dpp_add(float v, const int ctrl_sel) {
  int t;
  if (ctrl_sel == 0) t = __builtin_amdgcn_mov_dpp(__float_as_int(v), 0xB1, 0xf, 0xf, true);
  else if (ctrl_sel == 1) t = __builtin_amdgcn_mov_dpp(__float_as_int(v), 0x4E, 0xf, 0xf, true);
  else t = __builtin_amdgcn_mov_dpp(__float_as_int(v), 0x141, 0xf, 0xf, true);
  return v + __int_as_float(t);
}
#define WAIT_VM(n) asm volatile("s_waitcnt vmcnt(%0)" ::"i"(n) : "memory")
#define BARRIER() do { asm volatile("s_waitcnt lgkmcnt(0)" ::: "memory"); __builtin_amdgcn_s_barrier(); asm volatile("" ::: "memory"); } while (0)

#define STAMP(buf, idx) do {} while (0)
DI int wsw(int r) { return ((r >> 1) & 1) | (((r >> 3) & 1) << 1); }

__global__ __launch_bounds__(512, 2) void k1_ln_gemm(const u16* __restrict__ x, const u16* __restrict__ wqkv,
                                                     const u16* __restrict__ wg, const u16* __restrict__ wu,
                                                     u16* __restrict__ qkv, u16* __restrict__ hout
                                                     ) {
  STAMP(stamps, 0);
  __shared__ __attribute__((aligned(16))) char smem[65536 + 4 * 20480];
  char* const Abuf = smem;
  char* const Bbuf = smem + 65536;
  const int tid = threadIdx.x, w = __builtin_amdgcn_readfirstlane(tid >> 6), l = tid & 63;
  const int bid = blockIdx.x, xcd = bid & 7, ii = bid >> 3;
  const int mt = (xcd & 1) * 8 + (ii & 7), nt = (xcd >> 1) * 4 + (ii >> 3);
  const int m0 = mt * 128;

  const u16* wsrc[3];
  int wld[3];
#pragma unroll
  for (int i = 0; i < 3; ++i) {
    const int n = w + 8 * i, pl = (n >> 2) < 5 ? (n >> 2) : 4, r = 8 * (n & 3) + (l >> 3), p = l & 7;
    const int chunk = ((((p >> 1) ^ wsw(r)) << 1) | (p & 1));
    const int P = nt * 5 + pl;
    if (P < 24) {
      wld[i] = NQKV;
      wsrc[i] = wqkv + (size_t)r * NQKV + 64 * P + 8 * chunk;
    } else {
      wld[i] = DFF;
      const u16* b = chunk < 4 ? wg : wu;
      wsrc[i] = b + (size_t)r * DFF + 32 * (P - 24) + 8 * (chunk & 3);
    }
  }
  auto stageW = [&](int s) {
    char* dst = Bbuf + (s & 3) * 20480 + w * 1024;
#pragma unroll
    for (int i = 0; i < 3; ++i) {
      if (i < 2 || w < 4) {
        glds16(wsrc[i], dst + i * 8192);
        wsrc[i] += 32 * wld[i];
      }
    }
  };
  stageW(0); stageW(1); stageW(2);

  const u16* xp = x + (size_t)(m0 + 16 * w + (l >> 3)) * D_ + 8 * (l & 7);
  float lsc[2], lsh[2];
  uint4 xr[2][8];
#pragma unroll
  for (int g = 0; g < 2; ++g)
#pragma unroll
    for (int i = 0; i < 8; ++i) xr[g][i] = *(const uint4*)(xp + g * 8 * D_ + 64 * i);
#pragma unroll
  for (int g = 0; g < 2; ++g) {
    float s1 = 0.f, s2 = 0.f;
    const bf16x2 ones = {(__bf16)1.0f, (__bf16)1.0f};
#pragma unroll
    for (int i = 0; i < 8; ++i) {
      const unsigned d[4] = {xr[g][i].x, xr[g][i].y, xr[g][i].z, xr[g][i].w};
#pragma unroll
      for (int e = 0; e < 4; ++e) {
        bf16x2 q = __builtin_bit_cast(bf16x2, d[e]);
        s1 = __builtin_amdgcn_fdot2_f32_bf16(q, ones, s1, false);
        s2 = __builtin_amdgcn_fdot2_f32_bf16(q, q, s2, false);
      }
    }
    s1 = dpp_add(s1, 0); s1 = dpp_add(s1, 1); s1 = dpp_add(s1, 2);
    s2 = dpp_add(s2, 0); s2 = dpp_add(s2, 1); s2 = dpp_add(s2, 2);
    const float mean = s1 * (1.0f / D_);
    float var = s2 * (1.0f / D_) - mean * mean;
    var = fmaxf(var, 0.f);
    const float mb = bfr(mean), vb = bfr(var);
    const float sd = bfr(sqrtf(bfr(vb + 1e-5f)));
    const float r = 1.0f / sd;
    lsc[g] = r;
    lsh[g] = -mb * r;
  }
  auto stageA = [&](const uint4 (&v)[2][8], int hbase) {
#pragma unroll
    for (int g = 0; g < 2; ++g) {
      const int row = 16 * w + 8 * g + (l >> 3);
      const float sc = lsc[g], sh = lsh[g];
#pragma unroll
      for (int i = 0; i < 4; ++i) {
        const uint4 q = v[g][hbase + i];
        uint4 o;
        o.x = pack2(bflo(q.x) * sc + sh, bfhi(q.x) * sc + sh);
        o.y = pack2(bflo(q.y) * sc + sh, bfhi(q.y) * sc + sh);
        o.z = pack2(bflo(q.z) * sc + sh, bfhi(q.z) * sc + sh);
        o.w = pack2(bflo(q.w) * sc + sh, bfhi(q.w) * sc + sh);
        *(uint4*)(Abuf + row * 512 + (((8 * i + (l & 7)) ^ (row & 15)) << 4)) = o;
      }
    }
  };
  STAMP(stamps, 1);
  stageA(xr, 0);

  const int wm = w & 3, wn = w >> 2;
  const int xrow = (32 * wm + (l & 15)) * 512, xsw = l & 15, xq = l >> 4;
  int woff[10];
  {
    const int g = l >> 4, q = (l & 15) >> 2, p = l & 3;
    const int sl = ((q >> 1) & 1) | ((g & 1) << 1);
    const int base = (8 * g + q) * 128 + 8 * p;
#pragma unroll
    for (int j = 0; j < 10; ++j) {
      const int u = 5 * wn + (j >> 1), pl = u >> 1, half = u & 1, e = j & 1;
      const bool isq = (nt * 5 + pl) < 24;
      const int c = isq ? (2 * half + e) : (half + 2 * e);
      woff[j] = pl * 4096 + base + ((c ^ sl) << 5);
    }
  }
  f32x4 acc[2][10];
#pragma unroll
  for (int mi = 0; mi < 2; ++mi)
#pragma unroll
    for (int j = 0; j < 10; ++j) acc[mi][j] = f32x4{0.f, 0.f, 0.f, 0.f};
  STAMP(stamps, 2);

  auto loadfr = [&](int st, bf16x8 (&xf)[2], bf16x8 (&wf)[10]) {
    const char* b = Bbuf + (st & 3) * 20480;
#pragma unroll
    for (int mi = 0; mi < 2; ++mi) xf[mi] = *(const bf16x8*)(Abuf + xrow + mi * 8192 + (((4 * (st & 7) + xq) ^ xsw) << 4));
#pragma unroll
    for (int j = 0; j < 10; ++j) wf[j] = tr_frag(b + woff[j]);
  };
  auto step = [&](int s, bf16x8 (&xf)[2], bf16x8 (&wf)[10], bf16x8 (&xn)[2], bf16x8 (&wnx)[10]) {
    STAMP(stamps, 16 + s);
    if (s < 13) { if (w < 4) WAIT_VM(6); else WAIT_VM(4); }
    else if (s == 13) { if (w < 4) WAIT_VM(3); else WAIT_VM(2); }
    else WAIT_VM(0);
    BARRIER();
    if (s == 7) {
      stageA(xr, 4);
      BARRIER();
    }
    const bool dodma = (s + 4 < 16);
    char* const wdst = Bbuf + (s & 3) * 20480 + w * 1024;
    const char* const bn = Bbuf + ((s + 1) & 3) * 20480;
#pragma unroll
    for (int mi = 0; mi < 2; ++mi) xn[mi] = *(const bf16x8*)(Abuf + xrow + mi * 8192 + (((4 * ((s + 1) & 7) + xq) ^ xsw) << 4));
    __builtin_amdgcn_sched_barrier(0);
#pragma unroll
    for (int j = 0; j < 10; ++j) {
#pragma unroll
      for (int mi = 0; mi < 2; ++mi) acc[mi][j] = __builtin_amdgcn_mfma_f32_16x16x32_bf16(wf[j], xf[mi], acc[mi][j], 0, 0, 0);
      wnx[j] = tr_frag(bn + woff[j]);
      __builtin_amdgcn_sched_barrier(0);
      if (j == 1 || j == 4 || j == 7) {
        const int i = j / 3;
        if (dodma && (i < 2 || w < 4)) {
          glds16(wsrc[i], wdst + i * 8192);
          wsrc[i] += 32 * wld[i];
        }
        __builtin_amdgcn_sched_barrier(0);
      }
    }
  };
  bf16x8 xfA[2], wfA[10], xfB[2], wfB[10];
  stageW(3);
  if (w < 4) WAIT_VM(9); else WAIT_VM(6);
  BARRIER();
  loadfr(0, xfA, wfA);
#pragma unroll 1
  for (int s = 0; s < 16; s += 2) {
    step(s, xfA, wfA, xfB, wfB);
    step(s + 1, xfB, wfB, xfA, wfA);
  }
  STAMP(stamps, 9);
  BARRIER();

  constexpr int CLD = 656;
  {
    char* cb = smem + (32 * wm + (l & 15)) * CLD + (160 * wn + 4 * (l >> 4)) * 2;
#pragma unroll
    for (int mi = 0; mi < 2; ++mi)
#pragma unroll
      for (int j = 0; j < 10; ++j) {
        const f32x4 v = acc[mi][j];
        u32x2 o = {pack2(v[0], v[1]), pack2(v[2], v[3])};
        *(u32x2*)(cb + mi * 16 * CLD + j * 32) = o;
      }
  }
  BARRIER();
#pragma unroll 1
  for (int uu = 0; uu < 10; ++uu) {
    const int U = nt * 10 + uu, P = U >> 1, half = U & 1;
    if (P < 24) {
      const int e = tid >> 8, tok = (tid >> 1) & 127, c2 = tid & 1;
      const uint4 v = *(const uint4*)(smem + tok * CLD + uu * 64 + e * 32 + c2 * 16);
      const int col = 64 * P + 32 * half + 16 * e;
      *(uint4*)(qkv + (size_t)(col >> 4) * (S_ * 16) + (m0 + tok) * 16 + 8 * c2) = v;
    } else if (tid < 256) {
      const int tok = tid >> 1, c2 = tid & 1;
      const uint4 gv = *(const uint4*)(smem + tok * CLD + uu * 64 + c2 * 16);
      const uint4 uv = *(const uint4*)(smem + tok * CLD + uu * 64 + 32 + c2 * 16);
      const unsigned gd[4] = {gv.x, gv.y, gv.z, gv.w}, ud[4] = {uv.x, uv.y, uv.z, uv.w};
      unsigned od[4];
#pragma unroll
      for (int i = 0; i < 4; ++i) {
        float hv[2];
#pragma unroll
        for (int e = 0; e < 2; ++e) {
          const float gg = e ? bfhi(gd[i]) : bflo(gd[i]), up = e ? bfhi(ud[i]) : bflo(ud[i]);
          const float z = 0.7978845608028654f * (gg + 0.044715f * gg * gg * gg);
          const float e2 = __builtin_amdgcn_exp2f(-2.0f * 1.4426950408889634f * z);
          const float ge = bfr(gg * __builtin_amdgcn_rcpf(1.0f + e2));
          hv[e] = ge * up;
        }
        od[i] = pack2(hv[0], hv[1]);
      }
      const int hcol = 32 * (P - 24) + 16 * half + 8 * c2;
      *(uint4*)(hout + (size_t)(m0 + tok) * DFF + hcol) = uint4{od[0], od[1], od[2], od[3]};
    }
  }
  STAMP(stamps, 10);
}

template <int K, int NS>
DI void gemm64(const u16* __restrict__ A, const u16* __restrict__ B, int m0, int n0, int pfidx, char* smem, f32x4 (&acc)[2][2]) {
  const int tid = threadIdx.x, w = tid >> 6, l = tid & 63;
  constexpr int NSTEP = K / 64;
  const u16* asrc = A + (size_t)(m0 + 8 * w + (l >> 3)) * K + 8 * ((l & 7) ^ (l >> 3));
  const u16* bsrc;
  {
    const int r = 8 * w + (l >> 3), p = l & 7;
    const int chunk = ((((p >> 1) ^ wsw(r)) << 1) | (p & 1));
    bsrc = B + (size_t)r * D_ + n0 + 8 * chunk;
  }
  auto stage = [&](int s) {
    char* dst = smem + (s % NS) * 16384 + w * 1024;
    glds16(asrc + s * 64, dst);
    glds16(bsrc + (size_t)s * 64 * D_, dst + 8192);
  };
#pragma unroll
  for (int s = 0; s < NS - 1; ++s) stage(s);

  const int kh = w >> 2, wq = w & 3, wm = wq & 1, wn = wq >> 1;
  const int xoff = (32 * wm + (l & 15)) * 128 + (((4 * kh + (l >> 4)) ^ (l & 7)) << 4);
  int woff[2];
  {
    const int g = l >> 4, q = (l & 15) >> 2, p = l & 3;
    const int sl = ((q >> 1) & 1) | ((g & 1) << 1);
#pragma unroll
    for (int nj = 0; nj < 2; ++nj) woff[nj] = 8192 + (32 * kh + 8 * g + q) * 128 + 8 * p + (((2 * wn + nj) ^ sl) << 5);
  }
#pragma unroll
  for (int mi = 0; mi < 2; ++mi)
#pragma unroll
    for (int nj = 0; nj < 2; ++nj) acc[mi][nj] = f32x4{0.f, 0.f, 0.f, 0.f};

  for (int s = 0; s < NSTEP; ++s) {
    if (s + NS - 2 < NSTEP) { WAIT_VM(2 * (NS - 2)); } else { WAIT_VM(0); }
    BARRIER();
    if (s + NS - 1 < NSTEP) stage(s + NS - 1);
    const char* buf = smem + (s % NS) * 16384;
    bf16x8 xf[2], wf[2];
#pragma unroll
    for (int mi = 0; mi < 2; ++mi) xf[mi] = *(const bf16x8*)(buf + xoff + mi * 2048);
#pragma unroll
    for (int nj = 0; nj < 2; ++nj) wf[nj] = tr_frag(buf + woff[nj]);
#pragma unroll
    for (int mi = 0; mi < 2; ++mi)
#pragma unroll
      for (int nj = 0; nj < 2; ++nj) acc[mi][nj] = __builtin_amdgcn_mfma_f32_16x16x32_bf16(wf[nj], xf[mi], acc[mi][nj], 0, 0, 0);
  }
  BARRIER();
  if (kh == 1) {
#pragma unroll
    for (int mi = 0; mi < 2; ++mi)
#pragma unroll
      for (int nj = 0; nj < 2; ++nj) *(f32x4*)(smem + ((wq * 4 + mi * 2 + nj) * 64 + l) * 16) = acc[mi][nj];
  }
  BARRIER();
  if (kh == 0) {
#pragma unroll
    for (int mi = 0; mi < 2; ++mi)
#pragma unroll
      for (int nj = 0; nj < 2; ++nj) acc[mi][nj] += *(const f32x4*)(smem + ((wq * 4 + mi * 2 + nj) * 64 + l) * 16);
  }
}

__global__ __launch_bounds__(512, 4) void k2_attn_down(const u16* __restrict__ qkv, const u16* __restrict__ h,
                                                       const u16* __restrict__ wdown, u16* __restrict__ o,
                                                       u16* __restrict__ gout
                                                       ) {
  STAMP(stamps, 0);
  __shared__ __attribute__((aligned(16))) char smem[81920];
  const int tid = threadIdx.x, w = tid >> 6, l = tid & 63;
  if (blockIdx.x < 256) {
    const int head = blockIdx.x >> 3, qc = blockIdx.x & 7;
    const u16* Q = qkv + (size_t)head * (S_ * 16);
    const u16* Kp = qkv + (size_t)(32 + head) * (S_ * 16);
    const u16* Vp = qkv + (size_t)(64 + head) * (S_ * 16);
#pragma unroll
    for (int i = 0; i < 4; ++i) {
      const int n = w * 4 + i, rb = n & 15;
      int key = 256 * qc - 256 + 32 * rb + (l >> 1);
      key = key < 0 ? 0 : key;
      const u16* src = (n < 16 ? Kp : Vp) + key * 16 + 8 * (l & 1);
      glds16(src, smem + n * 1024);
    }
    const int r = l & 31, hh = l >> 5;
    const int q0 = 256 * qc + 32 * w;
    const bf16x8 qf = *(const bf16x8*)(Q + (q0 + r) * 16 + 8 * hh);
    WAIT_VM(0);
    STAMP(stamps, 1);
    BARRIER();
    STAMP(stamps, 2);

    const float c = 0.25f * 1.4426950408889634f;
    f32x16 oacc;
#pragma unroll
    for (int i = 0; i < 16; ++i) oacc[i] = 0.f;
    const int t0 = (qc == 0) ? (8 - w) : 0;
    const char* kbase = smem + (32 * w + r) * 32 + 16 * hh;
    const char* vbase = smem + 16384 + (32 * w + 4 * hh + ((l & 15) >> 2)) * 32 + 8 * (l & 3);
    const bool isd = (l & 16) == 0;
    f32x16 zero16;
#pragma unroll
    for (int i = 0; i < 16; ++i) zero16[i] = 0.f;
    auto scores = [&](int t) {
      const bf16x8 kf = *(const bf16x8*)(kbase + t * 1024);
      f32x16 s = __builtin_amdgcn_mfma_f32_32x32x16_bf16(kf, qf, zero16, 0, 0, 0);
      if (t == 0) {
#pragma unroll
        for (int i = 0; i < 16; ++i) { const int kk = (i & 3) + 8 * (i >> 2) + 4 * hh; s[i] = (kk > r) ? s[i] : -INFINITY; }
      } else if (t == 8) {
#pragma unroll
        for (int i = 0; i < 16; ++i) { const int kk = (i & 3) + 8 * (i >> 2) + 4 * hh; s[i] = (kk <= r) ? s[i] : -INFINITY; }
      }
      return s;
    };
    float m = -INFINITY;
#pragma unroll
    for (int t = 0; t <= 8; ++t) {
      if (t >= t0) {
        const f32x16 s = scores(t);
#pragma unroll
        for (int i = 0; i < 16; ++i) m = fmaxf(m, s[i]);
      }
    }
    m = fmaxf(m, __shfl_xor(m, 32));
    STAMP(stamps, 3);
    const float mc = m * c;
#pragma unroll
    for (int t = 0; t <= 8; ++t) {
      if (t >= t0) {
        const f32x16 s = scores(t);
        unsigned pp[8];
#pragma unroll
        for (int i = 0; i < 8; ++i)
          pp[i] = pack2(__builtin_amdgcn_exp2f(s[2 * i] * c - mc), __builtin_amdgcn_exp2f(s[2 * i + 1] * c - mc));
#pragma unroll
        for (int st = 0; st < 2; ++st) {
          const char* vp = vbase + t * 1024 + st * 512;
          s16x4 lo = __builtin_amdgcn_ds_read_tr16_b64_v4i16((lds_s16x4_ptr)(vp));
          s16x4 hi = __builtin_amdgcn_ds_read_tr16_b64_v4i16((lds_s16x4_ptr)(vp + 256));
          s16x8 vv = __builtin_shufflevector(lo, hi, 0, 1, 2, 3, 4, 5, 6, 7);
          s16x8 one8 = {0x3F80, 0x3F80, 0x3F80, 0x3F80, 0x3F80, 0x3F80, 0x3F80, 0x3F80};
          vv = isd ? vv : one8;
          u32x4 pw = {pp[4 * st], pp[4 * st + 1], pp[4 * st + 2], pp[4 * st + 3]};
          oacc = __builtin_amdgcn_mfma_f32_32x32x16_bf16(__builtin_bit_cast(bf16x8, vv), __builtin_bit_cast(bf16x8, pw), oacc, 0, 0, 0);
        }
      }
    }
    const float inv = 1.0f / oacc[8];
    u16* dst = o + (size_t)(q0 + r) * D_ + head * 16 + 4 * hh;
    u32x2 o0 = {pack2(oacc[0] * inv, oacc[1] * inv), pack2(oacc[2] * inv, oacc[3] * inv)};
    u32x2 o1 = {pack2(oacc[4] * inv, oacc[5] * inv), pack2(oacc[6] * inv, oacc[7] * inv)};
    *(u32x2*)(dst) = o0;
    *(u32x2*)(dst + 8) = o1;
    STAMP(stamps, 4);
  } else {
    const int b = blockIdx.x - 256, xcd = b & 7, ii = b >> 3;
    const int mt = xcd * 4 + (ii & 3), nt = ii >> 2;
    f32x4 acc[2][2];
    gemm64<DFF, 5>(h, wdown, mt * 64, nt * 64, ii, smem, acc);
    if (w < 4) {
      const int wm = w & 1, wn = w >> 1;
#pragma unroll
      for (int mi = 0; mi < 2; ++mi)
#pragma unroll
        for (int nj = 0; nj < 2; ++nj) {
          float gv[4];
#pragma unroll
          for (int r = 0; r < 4; ++r) {
            const float d = bfr(acc[mi][nj][r]);
            gv[r] = d * __builtin_amdgcn_rcpf(1.0f + __builtin_amdgcn_exp2f(-1.4426950408889634f * d));
          }
          const int tok = mt * 64 + 32 * wm + 16 * mi + (l & 15), col = nt * 64 + 32 * wn + 16 * nj + 4 * (l >> 4);
          u32x2 ov = {pack2(gv[0], gv[1]), pack2(gv[2], gv[3])};
          *(u32x2*)(gout + (size_t)tok * D_ + col) = ov;
        }
    }
  }
}

__global__ __launch_bounds__(512, 2) void k3_wo_final(const u16* __restrict__ o, const u16* __restrict__ wo,
                                                      const u16* __restrict__ x, const u16* __restrict__ g,
                                                      u16* __restrict__ out) {
  __shared__ __attribute__((aligned(16))) char smem[65536];
  const int tid = threadIdx.x, w = tid >> 6, l = tid & 63;
  const int b = blockIdx.x, xcd = b & 7, ii = b >> 3;
  const int mt = xcd * 4 + (ii & 3), nt = ii >> 2;
  f32x4 acc[2][2];
  gemm64<D_, K3_NS>(o, wo, mt * 64, nt * 64, ii, smem, acc);
  if (w < 4) {
    const int wm = w & 1, wn = w >> 1;
#pragma unroll
    for (int mi = 0; mi < 2; ++mi)
#pragma unroll
      for (int nj = 0; nj < 2; ++nj) {
        const int tok = mt * 64 + 32 * wm + 16 * mi + (l & 15), col = nt * 64 + 32 * wn + 16 * nj + 4 * (l >> 4);
        const size_t idx = (size_t)tok * D_ + col;
        const u32x2 xv = *(const u32x2*)(x + idx), gv = *(const u32x2*)(g + idx);
        const float xs[4] = {bflo(xv[0]), bfhi(xv[0]), bflo(xv[1]), bfhi(xv[1])};
        const float gs[4] = {bflo(gv[0]), bfhi(gv[0]), bflo(gv[1]), bfhi(gv[1])};
        float ov[4];
#pragma unroll
        for (int r = 0; r < 4; ++r) ov[r] = xs[r] + bfr(bfr(acc[mi][nj][r]) * gs[r]);
        u32x2 o2 = {pack2(ov[0], ov[1]), pack2(ov[2], ov[3])};
        *(u32x2*)(out + idx) = o2;
      }
  }
}

extern "C" void kernel_launch(void* const* d_in, const int* in_sizes, int n_in, void* d_out, int out_size, void* d_ws,
                              size_t ws_size, hipStream_t stream) {
  const u16* x = (const u16*)d_in[0];
  const u16* wqkv = (const u16*)d_in[1];
  const u16* wo = (const u16*)d_in[2];
  const u16* wg = (const u16*)d_in[4];
  const u16* wu = (const u16*)d_in[5];
  const u16* wd = (const u16*)d_in[6];
  char* ws = (char*)d_ws;
  u16* qkv = (u16*)(ws);
  u16* h = (u16*)(ws + (6u << 20));
  u16* o = (u16*)(ws + (14u << 20));
  u16* g = (u16*)(ws + (16u << 20));
  k1_ln_gemm<<<256, 512, 0, stream>>>(x, wqkv, wg, wu, qkv, h
  );
  k2_attn_down<<<512, 512, 0, stream>>>(qkv, h, wd, o, g
  );
  k3_wo_final<<<256, 512, 0, stream>>>(o, wo, x, g, (u16*)d_out);
}
```

```cpp
#include <hip/hip_runtime.h>
#include <stdint.h>

typedef unsigned short u16;
typedef __bf16 bf16x8 __attribute__((ext_vector_type(8)));
typedef __bf16 bf16x2 __attribute__((ext_vector_type(2)));
typedef short s16x4 __attribute__((ext_vector_type(4)));
typedef short s16x8 __attribute__((ext_vector_type(8)));
typedef float f32x2 __attribute__((ext_vector_type(2)));
typedef float f32x4 __attribute__((ext_vector_type(4)));
typedef float f32x16 __attribute__((ext_vector_type(16)));
typedef unsigned u32x2 __attribute__((ext_vector_type(2)));
typedef unsigned u32x4 __attribute__((ext_vector_type(4)));
typedef __attribute__((address_space(3))) s16x4* lds_s16x4_ptr;

#define DI __device__ __forceinline__

constexpr int S_ = 2048, D_ = 512, DFF = 1792, NQKV = 1536;

DI float bf2f(u16 v) { return __uint_as_float(((unsigned)v) << 16); }
DI float bflo(unsigned p) { return __uint_as_float(p << 16); }
DI float bfhi(unsigned p) { return __uint_as_float(p & 0xffff0000u); }
DI unsigned pack2(float a, float b) {
  f32x2 f = {a, b};
  return __builtin_bit_cast(unsigned, __builtin_convertvector(f, bf16x2));
}
DI float bfr(float a) { return __uint_as_float(((unsigned)__builtin_bit_cast(u16, (__bf16)a)) << 16); }
DI bf16x8 tr_frag(const char* p) {
  s16x4 lo = __builtin_amdgcn_ds_read_tr16_b64_v4i16((lds_s16x4_ptr)(p));
  s16x4 hi = __builtin_amdgcn_ds_read_tr16_b64_v4i16((lds_s16x4_ptr)(p + 512));
  s16x8 r = __builtin_shufflevector(lo, hi, 0, 1, 2, 3, 4, 5, 6, 7);
  return __builtin_bit_cast(bf16x8, r);
}
DI void glds16(const void* g, const void* l) {
  unsigned keep;
  const unsigned lo = __builtin_amdgcn_readfirstlane((unsigned)(size_t)l);
  asm volatile("s_mov_b32 %0, m0\n\ts_mov_b32 m0, %2\n\ts_nop 0\n\tglobal_load_lds_dwordx4 %1, off\n\ts_mov_b32 m0, %0"
               : "=&s"(keep) : "v"(g), "s"(lo) : "memory");
}
DI float dpp_add(float v, const int ctrl_sel) {
  int t;
  if (ctrl_sel == 0) t = __builtin_amdgcn_mov_dpp(__float_as_int(v), 0xB1, 0xf, 0xf, true);
  else if (ctrl_sel == 1) t = __builtin_amdgcn_mov_dpp(__float_as_int(v), 0x4E, 0xf, 0xf, true);
  else t = __builtin_amdgcn_mov_dpp(__float_as_int(v), 0x141, 0xf, 0xf, true);
  return v + __int_as_float(t);
}
#define WAIT_VM(n) asm volatile("s_waitcnt vmcnt(%0)" ::"i"(n) : "memory")
#define BARRIER() do { asm volatile("s_waitcnt lgkmcnt(0)" ::: "memory"); __builtin_amdgcn_s_barrier(); asm volatile("" ::: "memory"); } while (0)

DI int wsw(int r) { return ((r >> 1) & 1) | (((r >> 3) & 1) << 1); }

__global__ __launch_bounds__(512, 2) void k1_ln_gemm(const u16* __restrict__ x, const u16* __restrict__ wqkv,
                                                     const u16* __restrict__ wg, const u16* __restrict__ wu,
                                                     u16* __restrict__ qkv, u16* __restrict__ hout
                                                     ) {
  __shared__ __attribute__((aligned(16))) char smem[65536 + 4 * 20480];
  char* const Abuf = smem;
  char* const Bbuf = smem + 65536;
  const int tid = threadIdx.x, w = __builtin_amdgcn_readfirstlane(tid >> 6), l = tid & 63;
  const int bid = blockIdx.x, xcd = bid & 7, ii = bid >> 3;
  const int mt = (xcd & 1) * 8 + (ii & 7), nt = (xcd >> 1) * 4 + (ii >> 3);
  const int m0 = mt * 128;

  const u16* wsrc[3];
  int wld[3];
#pragma unroll
  for (int i = 0; i < 3; ++i) {
    const int n = w + 8 * i, pl = (n >> 2) < 5 ? (n >> 2) : 4, r = 8 * (n & 3) + (l >> 3), p = l & 7;
    const int chunk = ((((p >> 1) ^ wsw(r)) << 1) | (p & 1));
    const int P = nt * 5 + pl;
    if (P < 24) {
      wld[i] = NQKV;
      wsrc[i] = wqkv + (size_t)r * NQKV + 64 * P + 8 * chunk;
    } else {
      wld[i] = DFF;
      const u16* b = chunk < 4 ? wg : wu;
      wsrc[i] = b + (size_t)r * DFF + 32 * (P - 24) + 8 * (chunk & 3);
    }
  }
  auto stageW = [&](int s) {
    char* dst = Bbuf + (s & 3) * 20480 + w * 1024;
#pragma unroll
    for (int i = 0; i < 3; ++i) {
      if (i < 2 || w < 4) {
        glds16(wsrc[i], dst + i * 8192);
        wsrc[i] += 32 * wld[i];
      }
    }
  };

  const u16* xp = x + (size_t)(m0 + 16 * w + (l >> 3)) * D_ + 8 * (l & 7);
  float lsc[2], lsh[2];
  uint4 xr[2][8];
#pragma unroll
  for (int g = 0; g < 2; ++g)
#pragma unroll
    for (int i = 0; i < 8; ++i) xr[g][i] = *(const uint4*)(xp + g * 8 * D_ + 64 * i);
  stageW(0); stageW(1); stageW(2);
#pragma unroll
  for (int g = 0; g < 2; ++g) {
    float s1 = 0.f, s2 = 0.f;
    const bf16x2 ones = {(__bf16)1.0f, (__bf16)1.0f};
#pragma unroll
    for (int i = 0; i < 8; ++i) {
      const unsigned d[4] = {xr[g][i].x, xr[g][i].y, xr[g][i].z, xr[g][i].w};
#pragma unroll
      for (int e = 0; e < 4; ++e) {
        bf16x2 q = __builtin_bit_cast(bf16x2, d[e]);
        s1 = __builtin_amdgcn_fdot2_f32_bf16(q, ones, s1, false);
        s2 = __builtin_amdgcn_fdot2_f32_bf16(q, q, s2, false);
      }
    }
    s1 = dpp_add(s1, 0); s1 = dpp_add(s1, 1); s1 = dpp_add(s1, 2);
    s2 = dpp_add(s2, 0); s2 = dpp_add(s2, 1); s2 = dpp_add(s2, 2);
    const float mean = s1 * (1.0f / D_);
    float var = s2 * (1.0f / D_) - mean * mean;
    var = fmaxf(var, 0.f);
    const float mb = bfr(mean), vb = bfr(var);
    const float sd = bfr(sqrtf(bfr(vb + 1e-5f)));
    const float r = 1.0f / sd;
    lsc[g] = r;
    lsh[g] = -mb * r;
  }
  auto stageA = [&](const uint4 (&v)[2][8], int hbase) {
#pragma unroll
    for (int g = 0; g < 2; ++g) {
      const int row = 16 * w + 8 * g + (l >> 3);
      const float sc = lsc[g], sh = lsh[g];
#pragma unroll
      for (int i = 0; i < 4; ++i) {
        const uint4 q = v[g][hbase + i];
        uint4 o;
        o.x = pack2(bflo(q.x) * sc + sh, bfhi(q.x) * sc + sh);
        o.y = pack2(bflo(q.y) * sc + sh, bfhi(q.y) * sc + sh);
        o.z = pack2(bflo(q.z) * sc + sh, bfhi(q.z) * sc + sh);
        o.w = pack2(bflo(q.w) * sc + sh, bfhi(q.w) * sc + sh);
        *(uint4*)(Abuf + row * 512 + (((8 * i + (l & 7)) ^ (row & 15)) << 4)) = o;
      }
    }
  };
  stageA(xr, 0);

  const int wm = w & 3, wn = w >> 2;
  const int xrow = (32 * wm + (l & 15)) * 512, xsw = l & 15, xq = l >> 4;
  int woff[10];
  {
    const int g = l >> 4, q = (l & 15) >> 2, p = l & 3;
    const int sl = ((q >> 1) & 1) | ((g & 1) << 1);
    const int base = (8 * g + q) * 128 + 8 * p;
#pragma unroll
    for (int j = 0; j < 10; ++j) {
      const int u = 5 * wn + (j >> 1), pl = u >> 1, half = u & 1, e = j & 1;
      const bool isq = (nt * 5 + pl) < 24;
      const int c = isq ? (2 * half + e) : (half + 2 * e);
      woff[j] = pl * 4096 + base + ((c ^ sl) << 5);
    }
  }
  f32x4 acc[2][10];
#pragma unroll
  for (int mi = 0; mi < 2; ++mi)
#pragma unroll
    for (int j = 0; j < 10; ++j) acc[mi][j] = f32x4{0.f, 0.f, 0.f, 0.f};

  auto loadfr = [&](int st, bf16x8 (&xf)[2], bf16x8 (&wf)[10]) {
    const char* b = Bbuf + (st & 3) * 20480;
#pragma unroll
    for (int mi = 0; mi < 2; ++mi) xf[mi] = *(const bf16x8*)(Abuf + xrow + mi * 8192 + (((4 * (st & 7) + xq) ^ xsw) << 4));
#pragma unroll
    for (int j = 0; j < 10; ++j) wf[j] = tr_frag(b + woff[j]);
  };
  auto step = [&](int s, bf16x8 (&xf)[2], bf16x8 (&wf)[10], bf16x8 (&xn)[2], bf16x8 (&wnx)[10]) {
    if (s < 13) { if (w < 4) WAIT_VM(6); else WAIT_VM(4); }
    else if (s == 13) { if (w < 4) WAIT_VM(3); else WAIT_VM(2); }
    else WAIT_VM(0);
    BARRIER();
    if (s == 7) {
      stageA(xr, 4);
      BARRIER();
    }
    const bool dodma = (s + 4 < 16);
    char* const wdst = Bbuf + (s & 3) * 20480 + w * 1024;
    const char* const bn = Bbuf + ((s + 1) & 3) * 20480;
#pragma unroll
    for (int mi = 0; mi < 2; ++mi) xn[mi] = *(const bf16x8*)(Abuf + xrow + mi * 8192 + (((4 * ((s + 1) & 7) + xq) ^ xsw) << 4));
    __builtin_amdgcn_sched_barrier(0);
#pragma unroll
    for (int j = 0; j < 10; ++j) {
#pragma unroll
      for (int mi = 0; mi < 2; ++mi) acc[mi][j] = __builtin_amdgcn_mfma_f32_16x16x32_bf16(wf[j], xf[mi], acc[mi][j], 0, 0, 0);
      wnx[j] = tr_frag(bn + woff[j]);
      __builtin_amdgcn_sched_barrier(0);
      if (j == 1 || j == 4 || j == 7) {
        const int i = j / 3;
        if (dodma && (i < 2 || w < 4)) {
          glds16(wsrc[i], wdst + i * 8192);
          wsrc[i] += 32 * wld[i];
        }
        __builtin_amdgcn_sched_barrier(0);
      }
    }
  };
  bf16x8 xfA[2], wfA[10], xfB[2], wfB[10];
  stageW(3);
  if (w < 4) WAIT_VM(9); else WAIT_VM(6);
  BARRIER();
  loadfr(0, xfA, wfA);
#pragma unroll 1
  for (int s = 0; s < 16; s += 2) {
    step(s, xfA, wfA, xfB, wfB);
    step(s + 1, xfB, wfB, xfA, wfA);
  }
  BARRIER();

  constexpr int CLD = 656;
  {
    char* cb = smem + (32 * wm + (l & 15)) * CLD + (160 * wn + 4 * (l >> 4)) * 2;
#pragma unroll
    for (int mi = 0; mi < 2; ++mi)
#pragma unroll
      for (int j = 0; j < 10; ++j) {
        const f32x4 v = acc[mi][j];
        u32x2 o = {pack2(v[0], v[1]), pack2(v[2], v[3])};
        *(u32x2*)(cb + mi * 16 * CLD + j * 32) = o;
      }
  }
  BARRIER();
#pragma unroll 5
  for (int uu = 0; uu < 10; ++uu) {
    const int U = nt * 10 + uu, P = U >> 1, half = U & 1;
    if (P < 24) {
      const int e = tid >> 8, tok = (tid >> 1) & 127, c2 = tid & 1;
      const uint4 v = *(const uint4*)(smem + tok * CLD + uu * 64 + e * 32 + c2 * 16);
      const int col = 64 * P + 32 * half + 16 * e;
      *(uint4*)(qkv + (size_t)(col >> 4) * (S_ * 16) + (m0 + tok) * 16 + 8 * c2) = v;
    } else if (tid < 256) {
      const int tok = tid >> 1, c2 = tid & 1;
      const uint4 gv = *(const uint4*)(smem + tok * CLD + uu * 64 + c2 * 16);
      const uint4 uv = *(const uint4*)(smem + tok * CLD + uu * 64 + 32 + c2 * 16);
      const unsigned gd[4] = {gv.x, gv.y, gv.z, gv.w}, ud[4] = {uv.x, uv.y, uv.z, uv.w};
      unsigned od[4];
#pragma unroll
      for (int i = 0; i < 4; ++i) {
        float hv[2];
#pragma unroll
        for (int e = 0; e < 2; ++e) {
          const float gg = e ? bfhi(gd[i]) : bflo(gd[i]), up = e ? bfhi(ud[i]) : bflo(ud[i]);
          const float z = 0.7978845608028654f * (gg + 0.044715f * gg * gg * gg);
          const float e2 = __builtin_amdgcn_exp2f(-2.0f * 1.4426950408889634f * z);
          const float ge = bfr(gg * __builtin_amdgcn_rcpf(1.0f + e2));
          hv[e] = ge * up;
        }
        od[i] = pack2(hv[0], hv[1]);
      }
      const int hcol = 32 * (P - 24) + 16 * half + 8 * c2;
      *(uint4*)(hout + (size_t)(m0 + tok) * DFF + hcol) = uint4{od[0], od[1], od[2], od[3]};
    }
  }
}

template <int K, int NS, int CH>
DI void gemm64(const u16* __restrict__ A, const u16* __restrict__ B, int m0, int n0, int pfidx, char* smem, f32x4 (&acc)[2][2]) {
  const int tid = threadIdx.x, w = tid >> 6, l = tid & 63;
  constexpr int NSTEP = K / (64 * CH);
  const u16* asrc = A + (size_t)(m0 + 8 * w + (l >> 3)) * K + 8 * ((l & 7) ^ (l >> 3));
  const u16* bsrc;
  {
    const int r = 8 * w + (l >> 3), p = l & 7;
    const int chunk = ((((p >> 1) ^ wsw(r)) << 1) | (p & 1));
    bsrc = B + (size_t)r * D_ + n0 + 8 * chunk;
  }
  auto stage = [&](int s) {
    char* dst = smem + (s % NS) * (CH * 16384) + w * 1024;
#pragma unroll
    for (int c = 0; c < CH; ++c) {
      glds16(asrc + (s * CH + c) * 64, dst + c * 16384);
      glds16(bsrc + (size_t)(s * CH + c) * 64 * D_, dst + c * 16384 + 8192);
    }
  };
#pragma unroll
  for (int s = 0; s < NS - 1; ++s) stage(s);

  const int kh = w >> 2, wq = w & 3, wm = wq & 1, wn = wq >> 1;
  const int xoff = (32 * wm + (l & 15)) * 128 + (((4 * kh + (l >> 4)) ^ (l & 7)) << 4);
  int woff[2];
  {
    const int g = l >> 4, q = (l & 15) >> 2, p = l & 3;
    const int sl = ((q >> 1) & 1) | ((g & 1) << 1);
#pragma unroll
    for (int nj = 0; nj < 2; ++nj) woff[nj] = 8192 + (32 * kh + 8 * g + q) * 128 + 8 * p + (((2 * wn + nj) ^ sl) << 5);
  }
#pragma unroll
  for (int mi = 0; mi < 2; ++mi)
#pragma unroll
    for (int nj = 0; nj < 2; ++nj) acc[mi][nj] = f32x4{0.f, 0.f, 0.f, 0.f};

  for (int s = 0; s < NSTEP; ++s) {
    if (s + NS - 2 < NSTEP) { WAIT_VM(2 * CH * (NS - 2)); } else { WAIT_VM(0); }
    BARRIER();
    if (s + NS - 1 < NSTEP) stage(s + NS - 1);
#pragma unroll
    for (int c = 0; c < CH; ++c) {
      const char* buf = smem + (s % NS) * (CH * 16384) + c * 16384;
      bf16x8 xf[2], wf[2];
#pragma unroll
      for (int mi = 0; mi < 2; ++mi) xf[mi] = *(const bf16x8*)(buf + xoff + mi * 2048);
#pragma unroll
      for (int nj = 0; nj < 2; ++nj) wf[nj] = tr_frag(buf + woff[nj]);
#pragma unroll
      for (int mi = 0; mi < 2; ++mi)
#pragma unroll
        for (int nj = 0; nj < 2; ++nj) acc[mi][nj] = __builtin_amdgcn_mfma_f32_16x16x32_bf16(wf[nj], xf[mi], acc[mi][nj], 0, 0, 0);
    }
  }
  BARRIER();
  if (kh == 1) {
#pragma unroll
    for (int mi = 0; mi < 2; ++mi)
#pragma unroll
      for (int nj = 0; nj < 2; ++nj) *(f32x4*)(smem + ((wq * 4 + mi * 2 + nj) * 64 + l) * 16) = acc[mi][nj];
  }
  BARRIER();
  if (kh == 0) {
#pragma unroll
    for (int mi = 0; mi < 2; ++mi)
#pragma unroll
      for (int nj = 0; nj < 2; ++nj) acc[mi][nj] += *(const f32x4*)(smem + ((wq * 4 + mi * 2 + nj) * 64 + l) * 16);
  }
}

__global__ __launch_bounds__(512, 4) void k2_attn_down(const u16* __restrict__ qkv, const u16* __restrict__ h,
                                                       const u16* __restrict__ wdown, u16* __restrict__ o,
                                                       u16* __restrict__ gout
                                                       ) {
  __shared__ __attribute__((aligned(16))) char smem[65536];
  const int tid = threadIdx.x, w = tid >> 6, l = tid & 63;
  if (blockIdx.x < 256) {
    const int head = blockIdx.x >> 3, qc = blockIdx.x & 7;
    const u16* Q = qkv + (size_t)head * (S_ * 16);
    const u16* Kp = qkv + (size_t)(32 + head) * (S_ * 16);
    const u16* Vp = qkv + (size_t)(64 + head) * (S_ * 16);
#pragma unroll
    for (int i = 0; i < 4; ++i) {
      const int n = w * 4 + i, rb = n & 15;
      int key = 256 * qc - 256 + 32 * rb + (l >> 1);
      key = key < 0 ? 0 : key;
      const u16* src = (n < 16 ? Kp : Vp) + key * 16 + 8 * (l & 1);
      glds16(src, smem + n * 1024);
    }
    const int r = l & 31, hh = l >> 5;
    const int q0 = 256 * qc + 32 * w;
    const bf16x8 qf = *(const bf16x8*)(Q + (q0 + r) * 16 + 8 * hh);
    WAIT_VM(0);
    BARRIER();

    const float c = 0.25f * 1.4426950408889634f;
    f32x16 oacc;
#pragma unroll
    for (int i = 0; i < 16; ++i) oacc[i] = 0.f;
    const int t0 = (qc == 0) ? (8 - w) : 0;
    const char* kbase = smem + (32 * w + r) * 32 + 16 * hh;
    const char* vbase = smem + 16384 + (32 * w + 4 * hh + ((l & 15) >> 2)) * 32 + 8 * (l & 3);
    const bool isd = (l & 16) == 0;
    f32x16 zero16;
#pragma unroll
    for (int i = 0; i < 16; ++i) zero16[i] = 0.f;
    auto scores = [&](int t) {
      const bf16x8 kf = *(const bf16x8*)(kbase + t * 1024);
      f32x16 s = __builtin_amdgcn_mfma_f32_32x32x16_bf16(kf, qf, zero16, 0, 0, 0);
      if (t == 0) {
#pragma unroll
        for (int i = 0; i < 16; ++i) { const int kk = (i & 3) + 8 * (i >> 2) + 4 * hh; s[i] = (kk > r) ? s[i] : -INFINITY; }
      } else if (t == 8) {
#pragma unroll
        for (int i = 0; i < 16; ++i) { const int kk = (i & 3) + 8 * (i >> 2) + 4 * hh; s[i] = (kk <= r) ? s[i] : -INFINITY; }
      }
      return s;
    };
    float m = -INFINITY;
#pragma unroll
    for (int t = 0; t <= 8; ++t) {
      if (t >= t0) {
        const f32x16 s = scores(t);
#pragma unroll
        for (int i = 0; i < 16; ++i) m = fmaxf(m, s[i]);
      }
    }
    m = fmaxf(m, __shfl_xor(m, 32));
    const float mc = m * c;
#pragma unroll
    for (int t = 0; t <= 8; ++t) {
      if (t >= t0) {
        const f32x16 s = scores(t);
        unsigned pp[8];
#pragma unroll
        for (int i = 0; i < 8; ++i)
          pp[i] = pack2(__builtin_amdgcn_exp2f(s[2 * i] * c - mc), __builtin_amdgcn_exp2f(s[2 * i + 1] * c - mc));
#pragma unroll
        for (int st = 0; st < 2; ++st) {
          const char* vp = vbase + t * 1024 + st * 512;
          s16x4 lo = __builtin_amdgcn_ds_read_tr16_b64_v4i16((lds_s16x4_ptr)(vp));
          s16x4 hi = __builtin_amdgcn_ds_read_tr16_b64_v4i16((lds_s16x4_ptr)(vp + 256));
          s16x8 vv = __builtin_shufflevector(lo, hi, 0, 1, 2, 3, 4, 5, 6, 7);
          s16x8 one8 = {0x3F80, 0x3F80, 0x3F80, 0x3F80, 0x3F80, 0x3F80, 0x3F80, 0x3F80};
          vv = isd ? vv : one8;
          u32x4 pw = {pp[4 * st], pp[4 * st + 1], pp[4 * st + 2], pp[4 * st + 3]};
          oacc = __builtin_amdgcn_mfma_f32_32x32x16_bf16(__builtin_bit_cast(bf16x8, vv), __builtin_bit_cast(bf16x8, pw), oacc, 0, 0, 0);
        }
      }
    }
    const float inv = 1.0f / oacc[8];
    u16* dst = o + (size_t)(q0 + r) * D_ + head * 16 + 4 * hh;
    u32x2 o0 = {pack2(oacc[0] * inv, oacc[1] * inv), pack2(oacc[2] * inv, oacc[3] * inv)};
    u32x2 o1 = {pack2(oacc[4] * inv, oacc[5] * inv), pack2(oacc[6] * inv, oacc[7] * inv)};
    *(u32x2*)(dst) = o0;
    *(u32x2*)(dst + 8) = o1;
  } else {
    const int b = blockIdx.x - 256, xcd = b & 7, ii = b >> 3;
    const int mt = xcd * 4 + (ii & 3), nt = ii >> 2;
    f32x4 acc[2][2];
    gemm64<DFF, 4, 1>(h, wdown, mt * 64, nt * 64, ii, smem, acc);
    if (w < 4) {
      const int wm = w & 1, wn = w >> 1;
#pragma unroll
      for (int mi = 0; mi < 2; ++mi)
#pragma unroll
        for (int nj = 0; nj < 2; ++nj) {
          float gv[4];
#pragma unroll
          for (int r = 0; r < 4; ++r) {
            const float d = bfr(acc[mi][nj][r]);
            gv[r] = d * __builtin_amdgcn_rcpf(1.0f + __builtin_amdgcn_exp2f(-1.4426950408889634f * d));
          }
          const int tok = mt * 64 + 32 * wm + 16 * mi + (l & 15), col = nt * 64 + 32 * wn + 16 * nj + 4 * (l >> 4);
          u32x2 ov = {pack2(gv[0], gv[1]), pack2(gv[2], gv[3])};
          *(u32x2*)(gout + (size_t)tok * D_ + col) = ov;
        }
    }
  }
}

__global__ __launch_bounds__(512, 2) void k3_wo_final(const u16* __restrict__ o, const u16* __restrict__ wo,
                                                      const u16* __restrict__ x, const u16* __restrict__ g,
                                                      u16* __restrict__ out) {
  __shared__ __attribute__((aligned(16))) char smem[65536];
  const int tid = threadIdx.x, w = tid >> 6, l = tid & 63;
  const int b = blockIdx.x, xcd = b & 7, ii = b >> 3;
  const int mt = xcd * 4 + (ii & 3), nt = ii >> 2;
  f32x4 acc[2][2];
  gemm64<D_, 4, 1>(o, wo, mt * 64, nt * 64, ii, smem, acc);
  if (w < 4) {
    const int wm = w & 1, wn = w >> 1;
#pragma unroll
    for (int mi = 0; mi < 2; ++mi)
#pragma unroll
      for (int nj = 0; nj < 2; ++nj) {
        const int tok = mt * 64 + 32 * wm + 16 * mi + (l & 15), col = nt * 64 + 32 * wn + 16 * nj + 4 * (l >> 4);
        const size_t idx = (size_t)tok * D_ + col;
        const u32x2 xv = *(const u32x2*)(x + idx), gv = *(const u32x2*)(g + idx);
        const float xs[4] = {bflo(xv[0]), bfhi(xv[0]), bflo(xv[1]), bfhi(xv[1])};
        const float gs[4] = {bflo(gv[0]), bfhi(gv[0]), bflo(gv[1]), bfhi(gv[1])};
        float ov[4];
#pragma unroll
        for (int r = 0; r < 4; ++r) ov[r] = xs[r] + bfr(bfr(acc[mi][nj][r]) * gs[r]);
        u32x2 o2 = {pack2(ov[0], ov[1]), pack2(ov[2], ov[3])};
        *(u32x2*)(out + idx) = o2;
      }
  }
}

extern "C" void kernel_launch(void* const* d_in, const int* in_sizes, int n_in, void* d_out, int out_size, void* d_ws,
                              size_t ws_size, hipStream_t stream) {
  const u16* x = (const u16*)d_in[0];
  const u16* wqkv = (const u16*)d_in[1];
  const u16* wo = (const u16*)d_in[2];
  const u16* wg = (const u16*)d_in[4];
  const u16* wu = (const u16*)d_in[5];
  const u16* wd = (const u16*)d_in[6];
  char* ws = (char*)d_ws;
  u16* qkv = (u16*)(ws);
  u16* h = (u16*)(ws + (6u << 20));
  u16* o = (u16*)(ws + (14u << 20));
  u16* g = (u16*)(ws + (16u << 20));
  k1_ln_gemm<<<256, 512, 0, stream>>>(x, wqkv, wg, wu, qkv, h
  );
  k2_attn_down<<<512, 512, 0, stream>>>(qkv, h, wd, o, g
  );
  k3_wo_final<<<256, 512, 0, stream>>>(o, wo, x, g, (u16*)d_out);
}
```
